```python
import torch

def _softmax(x, axis=-1):
    return torch.softmax(x, dim=axis)
def _gelu(x):
    return torch.nn.functional.gelu(x, approximate="tanh")
def _silu(x):
    return torch.nn.functional.silu(x)
def _sigmoid(x):
    return torch.sigmoid(x)
def _layernorm(x, axis=-1, eps=1e-5):
    m = x.mean(dim=axis, keepdim=True)
    v = x.var(dim=axis, keepdim=True, unbiased=False)
    return (x - m) / torch.sqrt(v + eps)
def _scatter_add(tgt, idx, src, axis=0):
    return tgt.clone().index_add_(axis, idx, src)
def _segment_sum(src, seg, n):
    out = torch.zeros((n,) + src.shape[1:], dtype=src.dtype)
    return out.index_add_(0, seg, src)
def _pad(x, pw):
    flat = tuple(v for pair in reversed(pw) for v in pair)
    return torch.nn.functional.pad(x, flat)

kernel_name = 'gated_mix__sliding_block_cached__dense_activation_squared_relu__decode'

def setup_inputs():

    _w = (lambda a: (lambda t: t.to(torch.bfloat16) if t.is_floating_point() else t)(torch.as_tensor(a)))
    def _A_setup():
        B, Sq, D = 1, 1, 448
        Hq, Hkv, Sk, Dh = 16, 8, 65536, 256
        assert Hq % Hkv == 0, (Hq, Hkv)
        return {
            "x": _w((lambda *s: torch.randn(*s, dtype=torch.bfloat16) * 0.05)(B, Sq, D)),
            "k_cache": _w((lambda *s: torch.randn(*s, dtype=torch.bfloat16) * 3.2)(B, Hkv, Sk, Dh)),
            "v_cache": _w((lambda *s: torch.randn(*s, dtype=torch.bfloat16) * 1.6)(B, Hkv, Sk, Dh)),
            "wq": _w((lambda *s: torch.randn(*s, dtype=torch.bfloat16) * 3.2)(D, Hq * Dh) * (D ** -0.5)),
            "wo": _w((lambda *s: torch.randn(*s, dtype=torch.bfloat16) * 1.6)(Hq * Dh, D) * ((Hq * Dh) ** -0.5)),
        }
    _A_in = _A_setup()

    def _B_setup():
        B, S, D, F = 1, 1, 448, 1792
        return {
            "x": _w((lambda *s: torch.randn(*s, dtype=torch.bfloat16) * 0.05)(B, S, D)),
            "w1": _w((lambda *s: torch.randn(*s, dtype=torch.bfloat16) * 0.1)(D, F)),
            "b1": _w((lambda *s: torch.randn(*s, dtype=torch.bfloat16) * 0.05)(F)),
            "w2": _w((lambda *s: torch.randn(*s, dtype=torch.bfloat16) * 0.05)(F, D)),
            "b2": _w((lambda *s: torch.randn(*s, dtype=torch.bfloat16) * 0.05)(D)),
        }
    _B_in = _B_setup()

    return {
        "A_x": _A_in["x"],
        "A_k_cache": _A_in["k_cache"],
        "A_v_cache": _A_in["v_cache"],
        "A_wq": _A_in["wq"],
        "A_wo": _A_in["wo"],
        "B_x": _B_in["x"],
        "B_w1": _B_in["w1"],
        "B_b1": _B_in["b1"],
        "B_w2": _B_in["w2"],
        "B_b2": _B_in["b2"],
        "gate_w": _w((lambda *s: torch.randn(*s, dtype=torch.bfloat16) * 0.05)(448) * 16.0),
    }

def reference(A_x, A_k_cache, A_v_cache, A_wq, A_wo, B_x, B_w1, B_b1, B_w2, B_b2, gate_w):
    def _A_ref(x, k_cache, v_cache, wq, wo):
        B, Sq, D = x.shape
        Hq, Dh = 16, 256
        Hkv, Sk = k_cache.shape[1], k_cache.shape[2]
        W = min(4096, max(256, Sk // 4))
        rep = Hq // Hkv
        q = (lambda x, *d: x.permute(*d))((x @ wq).reshape(B, Sq, Hq, Dh), 0, 2, 1, 3)
        k = (lambda x, n, axis: torch.repeat_interleave(x, n, dim=axis))(k_cache[:, :, Sk - W:, :], rep, axis=1)
        v = (lambda x, n, axis: torch.repeat_interleave(x, n, dim=axis))(v_cache[:, :, Sk - W:, :], rep, axis=1)
        scores = torch.einsum("bhqd,bhkd->bhqk", q, k) * (Dh ** -0.5)
        p = _softmax(scores, axis=-1)
        o = torch.einsum("bhqk,bhkd->bhqd", p, v)
        return (lambda x, *d: x.permute(*d))(o, 0, 2, 1, 3).reshape(B, Sq, Hq * Dh) @ wo

    def _B_ref(x, w1, b1, w2, b2):
        h = torch.einsum("bsd,df->bsf", x, w1) + b1
        zero = torch.zeros_like(h)
        r = torch.where(h > zero, h, zero)
        h = r * r
        out = torch.einsum("bsf,fd->bsd", h, w2) + b2
        return out

    x = A_x
    g = _sigmoid((x * gate_w).sum(-1, keepdims=True))
    a = _A_ref(x, A_k_cache, A_v_cache, A_wq, A_wo)
    b = _B_ref(x, B_w1, B_b1, B_w2, B_b2)
    return g * a + (1 - g) * b
```

```cpp
#include <hip/hip_runtime.h>
#include <hip/hip_bf16.h>
#include <stdint.h>

typedef unsigned short u16;
typedef unsigned u32x4 __attribute__((ext_vector_type(4)));
constexpr int D = 448, HQ = 16, HKV = 8, SK = 65536, DH = 256, W = 4096, F = 1792;
constexpr int NSPLIT = 32, KPS = W / NSPLIT;

__device__ __forceinline__ float bf2f(u16 b) { return __uint_as_float(((unsigned)b) << 16); }
__device__ __forceinline__ float rbf(float f) {
  unsigned u = __float_as_uint(f); u += 0x7FFFu + ((u >> 16) & 1u); return __uint_as_float(u & 0xFFFF0000u);
}

__global__ void k1(const u16* x, const u16* wq, const u16* w1, const u16* b1, float* q, float* h) {
  int c = blockIdx.x * blockDim.x + threadIdx.x;
  if (c < HQ * DH) {
    float acc = 0.f;
    for (int r = 0; r < D; ++r) acc += bf2f(x[r]) * bf2f(wq[(size_t)r * (HQ * DH) + c]);
    q[c] = rbf(acc);
  } else if (c < HQ * DH + F) {
    int f = c - HQ * DH;
    float acc = 0.f;
    for (int r = 0; r < D; ++r) acc += bf2f(x[r]) * bf2f(w1[(size_t)r * F + f]);
    float hh = rbf(rbf(acc) + bf2f(b1[f]));
    hh = hh > 0.f ? hh : 0.f;
    h[f] = rbf(hh * hh);
  }
}

__global__ void k2(const u16* kc, const u16* vc, const float* q, float* po, float* pml) {
  int s = blockIdx.x, hk = blockIdx.y, t = threadIdx.x;
  __shared__ float sq[2][DH];
  __shared__ float sc[2][KPS];
  __shared__ float red[2][2];
  sq[0][t] = q[(hk * 2) * DH + t]; sq[1][t] = q[(hk * 2 + 1) * DH + t];
  __syncthreads();
  const u16* kb = kc + ((size_t)hk * SK + (SK - W) + s * KPS) * DH;
  const u16* vb = vc + ((size_t)hk * SK + (SK - W) + s * KPS) * DH;
  { int key = t & 127, hh = t >> 7; float acc = 0.f;
    for (int d = 0; d < DH; ++d) acc += sq[hh][d] * bf2f(kb[(size_t)key * DH + d]);
    sc[hh][key] = rbf(acc) * 0.0625f; }
  __syncthreads();
  if (t < 2) { float m = -INFINITY; for (int k = 0; k < KPS; ++k) m = fmaxf(m, sc[t][k]); float l = 0.f;
    for (int k = 0; k < KPS; ++k) { float p = __expf(sc[t][k] - m); sc[t][k] = p; l += p; }
    red[t][0] = m; red[t][1] = l; }
  __syncthreads();
  float a0 = 0.f, a1 = 0.f;
  for (int k = 0; k < KPS; ++k) { float v = bf2f(vb[(size_t)k * DH + t]); a0 += sc[0][k] * v; a1 += sc[1][k] * v; }
  po[(((size_t)(hk * 2) * NSPLIT) + s) * DH + t] = a0;
  po[(((size_t)(hk * 2 + 1) * NSPLIT) + s) * DH + t] = a1;
  if (t < 2) { pml[((hk * 2 + t) * NSPLIT + s) * 2] = red[t][0]; pml[((hk * 2 + t) * NSPLIT + s) * 2 + 1] = red[t][1]; }
}

__global__ void k3(const float* po, const float* pml, float* o) {
  int hq = blockIdx.x, t = threadIdx.x;
  float M = -INFINITY;
  for (int s = 0; s < NSPLIT; ++s) M = fmaxf(M, pml[(hq * NSPLIT + s) * 2]);
  float L = 0.f, acc = 0.f;
  for (int s = 0; s < NSPLIT; ++s) { float w = __expf(pml[(hq * NSPLIT + s) * 2] - M); L += w * pml[(hq * NSPLIT + s) * 2 + 1]; acc += w * po[((size_t)hq * NSPLIT + s) * DH + t]; }
  o[hq * DH + t] = rbf(acc / L);
}

__global__ void k4(const u16* x, const u16* gw, const float* o, const float* h, const u16* wo, const u16* w2, const u16* b2, __hip_bfloat16* out) {
  int j = blockIdx.x, t = threadIdx.x;
  __shared__ float r[3][256];
  float a = 0.f, b = 0.f, g = 0.f;
  for (int i = t; i < HQ * DH; i += 256) a += o[i] * bf2f(wo[(size_t)i * D + j]);
  for (int i = t; i < F; i += 256) b += h[i] * bf2f(w2[(size_t)i * D + j]);
  for (int i = t; i < D; i += 256) g += rbf(bf2f(x[i]) * bf2f(gw[i]));
  r[0][t] = a; r[1][t] = b; r[2][t] = g;
  __syncthreads();
  for (int st = 128; st > 0; st >>= 1) { if (t < st) { r[0][t] += r[0][t + st]; r[1][t] += r[1][t + st]; r[2][t] += r[2][t + st]; } __syncthreads(); }
  if (t == 0) {
    float gs = rbf(r[2][0]); float gg = rbf(1.f / (1.f + __expf(-gs)));
    float aa = rbf(r[0][0]); float bb = rbf(rbf(r[1][0]) + bf2f(b2[j]));
    float res = rbf(gg * aa) + rbf(rbf(1.f - gg) * bb);
    out[j] = __float2bfloat16(res);
  }
}

extern "C" void kernel_launch(void* const* d_in, const int* in_sizes, int n_in,
                              void* d_out, int out_size, void* d_ws, size_t ws_size,
                              hipStream_t stream) {
  const u16* x = (const u16*)d_in[0]; const u16* kc = (const u16*)d_in[1]; const u16* vc = (const u16*)d_in[2];
  const u16* wq = (const u16*)d_in[3]; const u16* wo = (const u16*)d_in[4];
  const u16* w1 = (const u16*)d_in[6]; const u16* b1 = (const u16*)d_in[7]; const u16* w2 = (const u16*)d_in[8]; const u16* b2 = (const u16*)d_in[9];
  const u16* gw = (const u16*)d_in[10];
  float* ws = (float*)d_ws;
  float* q = ws; float* h = q + 4096; float* o = h + 2048; float* pml = o + 4096; float* po = pml + 2048;
  k1<<<(HQ * DH + F + 63) / 64, 64, 0, stream>>>(x, wq, w1, b1, q, h);
  k2<<<dim3(NSPLIT, HKV), 256, 0, stream>>>(kc, vc, q, po, pml);
  k3<<<HQ, 256, 0, stream>>>(po, pml, o);
  k4<<<D, 256, 0, stream>>>(x, gw, o, h, wo, w2, b2, (__hip_bfloat16*)d_out);
}
```

```cpp
#include <hip/hip_runtime.h>
#include <hip/hip_bf16.h>
#include <stdint.h>

typedef unsigned short u16;
typedef unsigned long long u64;
typedef unsigned v4u __attribute__((ext_vector_type(4)));
typedef unsigned v2u __attribute__((ext_vector_type(2)));
typedef float v2f __attribute__((ext_vector_type(2)));
typedef float v4f __attribute__((ext_vector_type(4)));
typedef short v4s __attribute__((ext_vector_type(4)));
typedef __bf16 bf16x2 __attribute__((ext_vector_type(2)));
typedef __bf16 bf16x8 __attribute__((ext_vector_type(8)));
#define GAS __attribute__((address_space(1)))
#define LAS __attribute__((address_space(3)))
#define RLX_AGENT __ATOMIC_RELAXED, __HIP_MEMORY_SCOPE_AGENT
#define RLX_WG __ATOMIC_RELAXED, __HIP_MEMORY_SCOPE_WORKGROUP

constexpr int D = 448, HQ = 16, SK = 65536, DH = 256, WIN = 4096, FF = 1792;
constexpr int NWG = 256, NTHR = 640;
constexpr int AUX_NT = 2, AUX_SC1 = 16;
constexpr unsigned RSRC3 = 0x00020000u, OOB = 0x40000000u, EMPTY = 0xffffffffu;
#define QDELAY 45
#define QSLEEP 24
#define MAXF 32
#define KDELAY 12
#define WSLEEP 8
#define WTIME 500
constexpr unsigned WS_EPOCH = 0;
constexpr unsigned WS_Q = 4096;
constexpr unsigned WS_H = WS_Q + 65536;
constexpr unsigned WS_O = 256u << 10;
constexpr unsigned WS_END = WS_O + (1u << 20);
constexpr int LB_K = 0, LB_V = 65536, LB_M = 131072;
constexpr int LB_QP = LB_M;
constexpr int LB_HP = LB_QP + 2048;
constexpr int LB_Q = LB_HP + 2048;
constexpr int LB_P = LB_Q + 8 * 528;
constexpr int LB_Z = LB_P + 544;
constexpr int LB_ML = LB_Z + 256;
constexpr int LB_FLG = LB_ML + 64;
constexpr int F_VGO = 0, F_Q = 1, F_QP = 2, F_LANDED = 3, F_G = 4, F_FIN = 5, F_ODONE = 6;
constexpr int LB_FIN = LB_FLG + 64;
constexpr int LB_MO = LB_FIN + 1152;
constexpr int LB_SCRAP = LB_MO + 1808;
constexpr int LB_TOTAL = LB_SCRAP + 256;
constexpr unsigned SPIN_CAP = 1u << 18;

struct Args { const u16 *x, *wq, *w1; unsigned char* ws; const u16 *kc, *vc, *wo, *w2, *b1, *b2, *gw; u16* out; };

__device__ __forceinline__ float bflo(unsigned v) { return __uint_as_float(v << 16); }
__device__ __forceinline__ float bfhi(unsigned v) { return __uint_as_float(v & 0xffff0000u); }
__device__ __forceinline__ unsigned cvtpk(float lo, float hi) { bf16x2 r = {(__bf16)lo, (__bf16)hi}; return __builtin_bit_cast(unsigned, r); }
__device__ __forceinline__ float rbf(float f) { return bflo(cvtpk(f, 0.f)); }
__device__ __forceinline__ float dot2(unsigned a, unsigned b, float acc) {
  return __builtin_amdgcn_fdot2_f32_bf16(__builtin_bit_cast(bf16x2, a), __builtin_bit_cast(bf16x2, b), acc, false);
}
__device__ __forceinline__ v4f mfma(v4u a, v4u b, v4f c) {
  return __builtin_amdgcn_mfma_f32_16x16x32_bf16(__builtin_bit_cast(bf16x8, a), __builtin_bit_cast(bf16x8, b), c, 0, 0, 0);
}
template <int CTRL> __device__ __forceinline__ unsigned dppu(unsigned x) { return (unsigned)__builtin_amdgcn_mov_dpp((int)x, CTRL, 0xf, 0xf, true); }
template <int CTRL> __device__ __forceinline__ float dpp(float x) { return __uint_as_float(dppu<CTRL>(__float_as_uint(x))); }
constexpr int XOR1 = 0xB1, XOR2 = 0x4E, XOR7 = 0x141, ROR8 = 0x128, ROR4 = 0x124, ROR2 = 0x122, ROR1 = 0x121;
__device__ __forceinline__ float tr32(float a, float b) {
  auto r = __builtin_amdgcn_permlane32_swap(__float_as_uint(a), __float_as_uint(b), false, false);
  return __uint_as_float(r[0]) + __uint_as_float(r[1]);
}
__device__ __forceinline__ float tr16(float a, float b) {
  auto r = __builtin_amdgcn_permlane16_swap(__float_as_uint(a), __float_as_uint(b), false, false);
  return __uint_as_float(r[0]) + __uint_as_float(r[1]);
}
template <int CTRL> __device__ __forceinline__ float trd(float lo, float hi, bool up) {
  const float t1 = lo + dpp<CTRL>(lo), t2 = hi + dpp<CTRL>(hi); return up ? t2 : t1;
}
__device__ __forceinline__ float red8(const float (&acc)[8], int lane) {
  float r4[4], r2[2];
#pragma unroll
  for (int e = 0; e < 4; ++e) r4[e] = tr32(acc[e], acc[e + 4]);
#pragma unroll
  for (int e = 0; e < 2; ++e) r2[e] = tr16(r4[e], r4[e + 2]);
  return trd<ROR8>(r2[0], r2[1], (lane & 8) != 0);
}
__device__ __forceinline__ __amdgpu_buffer_rsrc_t mkrs(const void* p, unsigned bytes) { return __builtin_amdgcn_make_buffer_rsrc((void*)p, 0, bytes, RSRC3); }
__device__ __forceinline__ v4u mksrd(const void* p, unsigned bytes) { const u64 q = (u64)p; return v4u{(unsigned)q, (unsigned)(q >> 32) & 0xffffu, bytes, RSRC3}; }
#define LDNT(rs, vo, so) __builtin_amdgcn_raw_buffer_load_b128(rs, vo, so, AUX_NT)
#define LDSC(rs, vo, so) __builtin_amdgcn_raw_buffer_load_b128(rs, vo, so, AUX_SC1)
#define STSC(v, rs, vo, so) __builtin_amdgcn_raw_buffer_store_b128(v, rs, vo, so, AUX_SC1)
__device__ __forceinline__ void dma(v4u srd, unsigned voff, unsigned soff, unsigned lds) {
  unsigned keep;
  asm volatile("s_mov_b32 %0, m0\n\ts_mov_b32 m0, %4\n\ts_nop 0\n\tbuffer_load_dwordx4 %1, %2, %3 offen nt lds\n\ts_mov_b32 m0, %0"
               : "=&s"(keep) : "v"(voff), "s"(srd), "s"(soff), "s"(lds) : "memory");
}
#define STR2(x) #x
#define STR(x) STR2(x)
#define VMWAIT(n) asm volatile("s_waitcnt vmcnt(" #n ")" ::: "memory")
#define CBAR() asm volatile("" ::: "memory")
template <bool SLEEP> __device__ __forceinline__ void lds_wait_ge(LAS unsigned* p, unsigned want) {
  unsigned spins = 0;
#pragma nounroll
  while ((unsigned)__builtin_amdgcn_readfirstlane(__hip_atomic_load(p, RLX_WG)) < want) { if (SLEEP) __builtin_amdgcn_s_sleep(1); if (++spins > SPIN_CAP * 8u) break; }
  CBAR();
}
__device__ __forceinline__ void lds_wait_or_time(LAS unsigned* p, unsigned want, unsigned t0, unsigned ticks) {
#pragma nounroll
  while ((unsigned)__builtin_amdgcn_readfirstlane(__hip_atomic_load(p, RLX_WG)) < want && (unsigned)__builtin_amdgcn_s_memrealtime() - t0 < ticks) __builtin_amdgcn_s_sleep(2);
  CBAR();
}
__device__ __forceinline__ void lds_set(LAS unsigned* p, unsigned v, int lane) {
  CBAR();
  if (lane == 0) __hip_atomic_store(p, v, RLX_WG);
  CBAR();
}
__device__ __forceinline__ unsigned lds_ticket(LAS unsigned* p, int lane) {
  CBAR();
  unsigned t = 0;
  if (lane == 0) t = __hip_atomic_fetch_add(p, 1u, RLX_WG);
  t = __builtin_amdgcn_readfirstlane(t);
  CBAR();
  return t;
}
__device__ __forceinline__ unsigned mix(unsigned e) { unsigned t = (e ^ 0x5bd1e995u) * 0x9E3779B1u; t ^= t >> 15; t *= 0x85EBCA77u; return t ^ (t >> 13); }
__device__ __forceinline__ float vmax(float a, float b) { float r; asm("v_max_f32 %0, %1, %2" : "=v"(r) : "v"(a), "v"(b)); return r; }
__device__ __forceinline__ float vmax3(float a, float b, float c) { float r; asm("v_max3_f32 %0, %1, %2, %3" : "=v"(r) : "v"(a), "v"(b), "v"(c)); return r; }
__device__ __forceinline__ float swapmax16(float x) { auto s = __builtin_amdgcn_permlane16_swap(__float_as_uint(x), __float_as_uint(x), false, false); return vmax(__uint_as_float(s[0]), __uint_as_float(s[1])); }
__device__ __forceinline__ float swapmax32(float x) { auto s = __builtin_amdgcn_permlane32_swap(__float_as_uint(x), __float_as_uint(x), false, false); return vmax(__uint_as_float(s[0]), __uint_as_float(s[1])); }
__device__ __forceinline__ bool all_tagged(const v4u (&g)[4], unsigned tag) {
  unsigned t = g[0].y ^ tag;
  t = __builtin_amdgcn_bitop3_b32(g[0].w, tag, t, 0xbe);
#pragma unroll
  for (int i = 1; i < 4; ++i) { t = __builtin_amdgcn_bitop3_b32(g[i].y, tag, t, 0xbe); t = __builtin_amdgcn_bitop3_b32(g[i].w, tag, t, 0xbe); }
  return __builtin_amdgcn_ballot_w64(t != 0u) == 0ull;
}
template <int STEP> __device__ __forceinline__ void gather4(__amdgpu_buffer_rsrc_t rw, unsigned off, unsigned tag, v4u (&g)[4]) {
  unsigned spins = 0;
#pragma nounroll
  do {
    CBAR();
#pragma unroll
    for (int i = 0; i < 4; ++i) g[i] = LDSC(rw, off + i * STEP, 0);
  } while (!all_tagged(g, tag) && ++spins < SPIN_CAP);
}

__global__ void __launch_bounds__(NTHR, 3) fused_decode(Args a) {
  __shared__ __attribute__((aligned(1024))) unsigned char lds_raw[LB_TOTAL];
  LAS unsigned char* LB = (LAS unsigned char*)lds_raw;
  LAS unsigned* FL = (LAS unsigned*)(LB + LB_FLG);
  __builtin_amdgcn_sched_barrier(0);
  const int lane = threadIdx.x & 63, wave = __builtin_amdgcn_readfirstlane(threadIdx.x >> 6);
  const int b = blockIdx.x, h = b & 7, s = b >> 3;
  const int rg = lane >> 3, c8 = lane & 7;
  const unsigned ldsbase = (unsigned)(size_t)LB;
  {
    u64 pc0; unsigned keep;
    asm volatile("s_getpc_b64 %0" : "=s"(pc0));
    const unsigned po = (wave * 9 + (lane < 9 ? lane : 8)) * 128;
    asm volatile("s_mov_b32 %0, m0\n\ts_mov_b32 m0, %3\n\ts_nop 0\n\tglobal_load_lds_dword %1, %2\n\ts_mov_b32 m0, %0"
                 : "=&s"(keep) : "v"(po), "s"(pc0 & ~255ull), "s"(ldsbase + LB_SCRAP) : "memory");
  }
  const int w = wave;
  const int cg1 = b % 28, rg1 = b / 28;
  const size_t kvrow = (size_t)h * SK + (SK - WIN);
  ((LAS unsigned*)(LB + LB_Z))[lane] = 0u;
  ((LAS unsigned*)(LB + LB_ML))[lane & 31] = (lane & 16) ? 0u : EMPTY;
  unsigned xv, x1v, epoch; v4u wa, wb, wc;
  const bool okq = rg < 7 && w < 8, ok1 = b < 224 && w < 7;
  {
    const unsigned row0 = (112 * (s >> 3) + 14 * w + 2 * rg) * (okq ? 1u : 0u), row1 = ok1 ? 56 * rg1 + 8 * w + rg : 0;
    const unsigned oq = row0 * 8192 + h * 1024 + (s & 7) * 128 + c8 * 16, o1 = row1 * (FF * 2) + cg1 * 128 + c8 * 16;
    const u64 pk = (u64)a.kc, pv = (u64)a.vc;
    asm volatile(
        "s_cmp_eq_u32 %[wave], 9\n\t"
        "s_cbranch_scc1 .Lload%=\n\t"
        "global_load_dwordx4 %[wa], %[oq], %[pwq] nt\n\t"
        "global_load_dwordx4 %[wb], %[oq2], %[pwq] nt\n\t"
        "global_load_dword %[xv], %[ox], %[px]\n\t"
        "global_load_dwordx4 %[wc], %[o1], %[pw1] nt\n\t"
        "global_load_ushort %[x1], %[ox1], %[px]\n\t"
        "global_load_dword %[ep], %[oe], %[pws] sc1\n\t"
        "s_waitcnt lgkmcnt(0)\n\t"
        "s_barrier\n\t"
        "s_waitcnt vmcnt(0)\n\t"
        "s_branch .Lend%=\n\t"
        ".Lload%=:\n"
        "s_add_u32 s64, %[pk], %[off]\n\t"
        "s_addc_u32 s65, %[pkh], 0\n\t"
        "s_and_b32 s65, s65, 0xffff\n\t"
        "s_mov_b32 s66, %[nrec]\n\t"
        "s_mov_b32 s67, 0x20000\n\t"
        "s_add_u32 s68, %[pv], %[off]\n\t"
        "s_addc_u32 s69, %[pvh], 0\n\t"
        "s_and_b32 s69, s69, 0xffff\n\t"
        "s_mov_b32 s70, %[nrec]\n\t"
        "s_mov_b32 s71, 0x20000\n\t"
        "v_lshrrev_b32 v40, 5, %[lane]\n\t"
        "v_and_b32 v41, 31, %[lane]\n\t"
        "v_lshlrev_b32 v42, 9, v40\n\t"
        "v_xor_b32 v43, v41, v40\n\t"
        "v_lshl_or_b32 v44, v43, 4, v42\n\t"
        "v_lshlrev_b32 v43, 1, v40\n\t"
        "v_xor_b32 v43, v41, v43\n\t"
        "v_lshl_or_b32 v52, v43, 4, v42\n\t"
        "v_xor_b32 v45, 32, v44\n\t"
        "v_add_u32 v45, 31744, v45\n\t"
        "v_xor_b32 v46, 64, v44\n\t"
        "v_add_u32 v46, 63488, v46\n\t"
        "v_xor_b32 v47, 96, v44\n\t"
        "v_add_u32 v47, 95232, v47\n\t"
        "v_xor_b32 v48, 128, v44\n\t"
        "v_xor_b32 v49, 160, v44\n\t"
        "v_add_u32 v49, 31744, v49\n\t"
        "v_xor_b32 v50, 192, v44\n\t"
        "v_add_u32 v50, 63488, v50\n\t"
        "v_xor_b32 v51, 224, v44\n\t"
        "v_add_u32 v51, 95232, v51\n\t"
        "v_xor_b32 v53, 64, v52\n\t"
        "v_add_u32 v53, 31744, v53\n\t"
        "v_add_u32 v54, 63488, v52\n\t"
        "v_xor_b32 v55, 64, v52\n\t"
        "v_add_u32 v55, 95232, v55\n\t"
        "v_xor_b32 v56, 128, v52\n\t"
        "v_xor_b32 v57, 192, v52\n\t"
        "v_add_u32 v57, 31744, v57\n\t"
        "v_xor_b32 v58, 128, v52\n\t"
        "v_add_u32 v58, 63488, v58\n\t"
        "v_xor_b32 v59, 192, v52\n\t"
        "v_add_u32 v59, 95232, v59\n\t"
        "s_xor_b32 s75, %[s], 0\n\ts_and_b32 s75, s75, 7\n\ts_lshl_b32 s75, s75, 10\n\tv_add_u32 v44, s75, v44\n\tv_add_u32 v52, s75, v52\n\t"
        "s_xor_b32 s75, %[s], 1\n\ts_and_b32 s75, s75, 7\n\ts_lshl_b32 s75, s75, 10\n\tv_add_u32 v45, s75, v45\n\tv_add_u32 v53, s75, v53\n\t"
        "s_xor_b32 s75, %[s], 2\n\ts_and_b32 s75, s75, 7\n\ts_lshl_b32 s75, s75, 10\n\tv_add_u32 v46, s75, v46\n\tv_add_u32 v54, s75, v54\n\t"
        "s_xor_b32 s75, %[s], 3\n\ts_and_b32 s75, s75, 7\n\ts_lshl_b32 s75, s75, 10\n\tv_add_u32 v47, s75, v47\n\tv_add_u32 v55, s75, v55\n\t"
        "s_xor_b32 s75, %[s], 4\n\ts_and_b32 s75, s75, 7\n\ts_lshl_b32 s75, s75, 10\n\tv_add_u32 v48, s75, v48\n\tv_add_u32 v56, s75, v56\n\t"
        "s_xor_b32 s75, %[s], 5\n\ts_and_b32 s75, s75, 7\n\ts_lshl_b32 s75, s75, 10\n\tv_add_u32 v49, s75, v49\n\tv_add_u32 v57, s75, v57\n\t"
        "s_xor_b32 s75, %[s], 6\n\ts_and_b32 s75, s75, 7\n\ts_lshl_b32 s75, s75, 10\n\tv_add_u32 v50, s75, v50\n\tv_add_u32 v58, s75, v58\n\t"
        "s_xor_b32 s75, %[s], 7\n\ts_and_b32 s75, s75, 7\n\ts_lshl_b32 s75, s75, 10\n\tv_add_u32 v51, s75, v51\n\tv_add_u32 v59, s75, v59\n\t"
        "v_lshl_add_u32 v40, %[lane], 2, %[scrap]\n\t"
        "v_cmp_eq_u32 vcc, 0, %[lane]\n\t"
        "v_mov_b32 v41, %[fl]\n\t"
        "v_mov_b32 v42, %[fv]\n\t"
        "v_cndmask_b32 v41, v40, v41, vcc\n\t"
        "v_cndmask_b32 v42, v40, v42, vcc\n\t"
        "s_mov_b32 s72, 0\n\ts_mov_b32 s77, 0\n\t"
        "s_mov_b32 s73, %[base]\n\t"
        "s_mov_b32 s74, 8 - " STR(MAXF) "\n\t"
        "s_setprio 2\n\t"
        "s_waitcnt lgkmcnt(0)\n\t"
        "s_barrier\n\t"
        "s_sleep " STR(KDELAY) "\n\t"
        ".Lk%=:\n"
        "s_xor_b32 s78, %[s], s77\n\ts_and_b32 s78, s78, 24\n\ts_lshl_b32 s78, s78, 10\n\ts_add_u32 s77, s77, 8\n\t"
        "s_waitcnt vmcnt(" STR(MAXF) " - 8)\n\t"
        "s_mov_b32 m0, s73\n\t"
        "s_add_u32 s76, s72, s78\n\t"
        "s_add_u32 s73, s73, 4096\n\t"
        "buffer_load_dwordx4 v44, s[64:67], s76 offen nt lds\n\t"
        "buffer_load_dwordx4 v45, s[64:67], s76 offen offset:1024 nt lds\n\t"
        "buffer_load_dwordx4 v46, s[64:67], s76 offen offset:2048 nt lds\n\t"
        "buffer_load_dwordx4 v47, s[64:67], s76 offen offset:3072 nt lds\n\t"
        "s_add_u32 s72, s72, 0x20000\n\t"
        "s_mov_b32 m0, s73\n\t"
        "s_add_u32 s76, s72, s78\n\t"
        "s_add_u32 s73, s73, 4096\n\t"
        "buffer_load_dwordx4 v48, s[64:67], s76 offen nt lds\n\t"
        "buffer_load_dwordx4 v49, s[64:67], s76 offen offset:1024 nt lds\n\t"
        "buffer_load_dwordx4 v50, s[64:67], s76 offen offset:2048 nt lds\n\t"
        "buffer_load_dwordx4 v51, s[64:67], s76 offen offset:3072 nt lds\n\t"
        "s_add_u32 s72, s72, 0x20000\n\t"
        "s_add_i32 s74, s74, 8\n\t"
        "s_cmp_lt_u32 s72, 0x200000\n\t"
        "s_cbranch_scc1 .Lk%=\n\t"
        "s_mov_b32 s72, 0\n\ts_mov_b32 s77, 0\n\t"
        ".Lv%=:\n"
        "s_xor_b32 s78, %[s], s77\n\ts_and_b32 s78, s78, 24\n\ts_lshl_b32 s78, s78, 10\n\ts_add_u32 s77, s77, 8\n\t"
        "s_waitcnt vmcnt(" STR(MAXF) " - 8)\n\t"
        "v_mov_b32 v40, s74\n\t"
        "ds_write_b32 v41, v40\n\t"
        "s_mov_b32 m0, s73\n\t"
        "s_add_u32 s76, s72, s78\n\t"
        "s_add_u32 s73, s73, 4096\n\t"
        "buffer_load_dwordx4 v52, s[68:71], s76 offen nt lds\n\t"
        "buffer_load_dwordx4 v53, s[68:71], s76 offen offset:1024 nt lds\n\t"
        "buffer_load_dwordx4 v54, s[68:71], s76 offen offset:2048 nt lds\n\t"
        "buffer_load_dwordx4 v55, s[68:71], s76 offen offset:3072 nt lds\n\t"
        "s_add_u32 s72, s72, 0x20000\n\t"
        "s_mov_b32 m0, s73\n\t"
        "s_add_u32 s76, s72, s78\n\t"
        "s_add_u32 s73, s73, 4096\n\t"
        "buffer_load_dwordx4 v56, s[68:71], s76 offen nt lds\n\t"
        "buffer_load_dwordx4 v57, s[68:71], s76 offen offset:1024 nt lds\n\t"
        "buffer_load_dwordx4 v58, s[68:71], s76 offen offset:2048 nt lds\n\t"
        "buffer_load_dwordx4 v59, s[68:71], s76 offen offset:3072 nt lds\n\t"
        "s_add_u32 s72, s72, 0x20000\n\t"
        "s_add_i32 s74, s74, 8\n\t"
        "s_cmp_lt_u32 s72, 0x180000\n\t"
        "s_cbranch_scc1 .Lv%=\n\t"
        "v_mov_b32 v40, 1\n\t"
        "ds_write_b32 v42, v40\n\t"
        "s_waitcnt vmcnt(24)\n\tv_mov_b32 v40, 88\n\tds_write_b32 v41, v40\n\t"
        "s_waitcnt vmcnt(16)\n\tv_mov_b32 v40, 96\n\tds_write_b32 v41, v40\n\t"
        "s_waitcnt vmcnt(8)\n\tv_mov_b32 v40, 104\n\tds_write_b32 v41, v40\n\t"
        "s_waitcnt vmcnt(0)\n\t"
        "v_mov_b32 v40, 112\n\t"
        "ds_write_b32 v41, v40\n\t"
        "s_endpgm\n\t"
        ".Lend%=:\n"
        : [xv] "=&v"(xv), [wa] "=&v"(wa), [wb] "=&v"(wb), [wc] "=&v"(wc), [x1] "=&v"(x1v), [ep] "=&v"(epoch)
        : [oq] "v"(oq), [oq2] "v"(oq + 8192), [ox] "v"(row0 * 2), [o1] "v"(o1), [ox1] "v"(row1 * 2), [oe] "v"(WS_EPOCH),
          [pwq] "s"(a.wq), [pw1] "s"(a.w1), [px] "s"(a.x), [pws] "s"(a.ws), [wave] "s"(wave), [lane] "v"(lane),
          [pk] "s"((unsigned)pk), [pkh] "s"((unsigned)(pk >> 32)), [pv] "s"((unsigned)pv), [pvh] "s"((unsigned)(pv >> 32)),
          [off] "s"((unsigned)kvrow * 512u), [nrec] "s"((unsigned)(WIN * 512)), [s] "s"(s),
          [scrap] "s"(ldsbase + LB_SCRAP), [fl] "s"(ldsbase + LB_FLG + F_LANDED * 4), [fv] "s"(ldsbase + LB_FLG + F_VGO * 4), [base] "s"(ldsbase + LB_K)
        : "memory", "scc", "vcc", "v40", "v41", "v42", "v43", "v44", "v45", "v46", "v47", "v48", "v49", "v50", "v51", "v52", "v53", "v54", "v55", "v56", "v57", "v58", "v59", "s64", "s65", "s66", "s67", "s68", "s69", "s70", "s71", "s72", "s73", "s74", "s75", "s76", "s77", "s78");
  }
  epoch = __builtin_amdgcn_readfirstlane(epoch);
  const unsigned tstart = (unsigned)__builtin_amdgcn_s_memrealtime();
  const unsigned tag = mix(epoch);

  if (__builtin_expect(wave < 8, 1)) {
    {
      float acc[8], ac1[8];
      const float x0 = okq ? bflo(xv) : 0.f, x1 = okq ? bfhi(xv) : 0.f, x2 = ok1 ? bflo(x1v) : 0.f;
#pragma unroll
      for (int e = 0; e < 4; ++e) { acc[2 * e] = fmaf(x1, bflo(wb[e]), x0 * bflo(wa[e])); acc[2 * e + 1] = fmaf(x1, bfhi(wb[e]), x0 * bfhi(wa[e]));
        ac1[2 * e] = x2 * bflo(wc[e]); ac1[2 * e + 1] = x2 * bfhi(wc[e]); }
      const float r = red8(acc, lane), r1 = red8(ac1, lane);
      const int ci = w * 64 + c8 * 8 + ((lane >> 5) & 1) * 4 + ((lane >> 4) & 1) * 2 + ((lane >> 3) & 1);
      ((LAS float*)(LB + LB_QP))[ci] = r; ((LAS float*)(LB + LB_HP))[ci] = r1;
    }
    const __amdgpu_buffer_rsrc_t rw = mkrs(a.ws, WS_END);
    if (lds_ticket(FL + F_QP, lane) == 7u) {
      __builtin_amdgcn_s_setprio(3);
      float t = 0.f, t1 = 0.f;
#pragma unroll
      for (int j = 0; j < 8; ++j) { t += ((LAS float*)(LB + LB_QP))[j * 64 + lane]; t1 += ((LAS float*)(LB + LB_HP))[j * 64 + lane]; }
      const unsigned g = cvtpk(t, dpp<XOR1>(t)), gn = dppu<XOR2>(g);
      const v4u pc = {g, tag, gn, tag};
      STSC(pc, rw, (lane & 3) == 0 ? WS_Q + ((h * 8 + (s & 7)) * 4 + (s >> 3)) * 256 + (lane >> 2) * 16 : OOB, 0);
      lds_set(FL + F_QP, 9u, lane);
      const v2u hc = {__float_as_uint(t1), tag};
      __builtin_amdgcn_raw_buffer_store_b64(hc, rw, b < 224 ? WS_H + (cg1 * 8 + rg) * 512 + rg1 * 64 + c8 * 8 : OOB, 0, AUX_SC1);
      __builtin_amdgcn_s_setprio(0);
    }
    if (w < 4) {
      const int g4 = lane >> 4, n16 = lane & 15, hd = g4 & 1;
      __builtin_amdgcn_s_setprio(3);
      const v4u sv = mksrd(a.vc + (kvrow + 48 * 64) * DH + w * 64, (unsigned)((WIN - 48 * 64) * 512 - w * 128));
      const unsigned vl = (rg >> 1) * 32768 + (rg & 1) * 512 + ((c8 ^ (rg & 2)) << 4);
#define VLAST() do { _Pragma("unroll") for (int j = 0; j < 4; ++j) dma(sv, (vl ^ ((j & 1) << 6)) + (((s ^ (16 + 4 * j + (rg >> 1))) & 31) << 10), j * 131072, ldsbase + LB_V + 49152 + w * 4096 + j * 1024); } while (0)
      lds_wait_ge<true>(FL + F_LANDED, 64u);
      v4u kf[2][8];
      {
        const LAS unsigned char* ka = LB + LB_K + (w * 32 + n16) * 512;
#pragma unroll
        for (int t = 0; t < 8; ++t) { kf[0][t] = *(const LAS v4u*)(ka + (((4 * t + g4) ^ n16) << 4)); kf[1][t] = *(const LAS v4u*)(ka + 8192 + (((4 * t + g4) ^ n16) << 4)); }
#pragma unroll
        for (int u = 0; u < 2; ++u) asm volatile("" : "+v"(kf[u][0]), "+v"(kf[u][1]), "+v"(kf[u][2]), "+v"(kf[u][3]), "+v"(kf[u][4]), "+v"(kf[u][5]), "+v"(kf[u][6]), "+v"(kf[u][7]));
      }
      bool vdone = false;
      {
        unsigned spins = 0;
#pragma nounroll
        for (;;) {
          CBAR();
          const v2u f = *(const LAS v2u*)(LB + LB_FLG);
          const unsigned vgo = __builtin_amdgcn_readfirstlane(f.x), qrdy = __builtin_amdgcn_readfirstlane(f.y);
          if (vgo && !vdone) { VLAST(); vdone = true; }
          if (qrdy || ++spins > SPIN_CAP * 8u) break;
          __builtin_amdgcn_s_sleep(1);
        }
        CBAR();
      }
      float sl;
      {
        const LAS unsigned char* qa = LB + LB_Q + (n16 & 7) * 528 + g4 * 16;
        v4u qf[8];
#pragma unroll
        for (int t = 0; t < 8; ++t) qf[t] = *(const LAS v4u*)(qa + t * 64);
        __builtin_amdgcn_sched_barrier(0);
        v4f d0 = {0.f, 0.f, 0.f, 0.f}, d1 = {0.f, 0.f, 0.f, 0.f};
#pragma unroll
        for (int t = 0; t < 8; ++t) { d0 = mfma(qf[t], kf[0][t], d0); d1 = mfma(qf[t], kf[1][t], d1); }
        const v4f d = (g4 >> 1) ? d1 : d0;
        sl = rbf(tr16(d.x + d.z, d.y + d.w)) * (0.0625f * 1.4426950408889634f);
      }
      {
        float m = sl; m = vmax(m, dpp<ROR8>(m)); m = vmax(m, dpp<ROR4>(m)); m = vmax(m, dpp<ROR2>(m)); m = vmax(m, dpp<ROR1>(m));
        m = swapmax32(m);
        const float p = __builtin_amdgcn_exp2f(sl - m);
        *(LAS u16*)(LB + LB_P + hd * 272 + (w * 32 + (g4 >> 1) * 16 + n16) * 2) = (u16)cvtpk(p, 0.f);
        float l = p; l += dpp<ROR8>(l); l += dpp<ROR4>(l); l += dpp<ROR2>(l); l += dpp<ROR1>(l); l = tr32(l, l);
        CBAR();
        if ((lane & 47) == 0) *(LAS v2f*)(LB + LB_ML + w * 16 + hd * 8) = v2f{m, l};
      }
      if (!vdone) { lds_wait_ge<true>(FL + F_VGO, 1u); VLAST(); }
      {
        unsigned spins = 0;
#pragma nounroll
        do { CBAR(); } while (!__all(*(const LAS unsigned*)(LB + LB_ML + n16 * 4) != EMPTY) && ++spins < SPIN_CAP * 8u);
        CBAR();
      }
      v4u pa[4], pb[4]; float cs[4]; unsigned stat;
      {
        const int prow = (n16 >> 2) & 1;
        const LAS unsigned char* a0 = LB + ((n16 & 8) ? LB_Z : LB_P + prow * 272) + g4 * 16;
        const LAS unsigned char* a1 = LB + ((n16 & 8) ? LB_P + prow * 272 : LB_Z) + g4 * 16;
        v2f ml[4];
#pragma unroll
        for (int t = 0; t < 4; ++t) { ml[t] = *(const LAS v2f*)(LB + LB_ML + t * 16 + hd * 8); pa[t] = *(const LAS v4u*)(a0 + t * 64); pb[t] = *(const LAS v4u*)(a1 + t * 64); }
        const float M = vmax(vmax3(ml[0].x, ml[1].x, ml[2].x), ml[3].x);
        float Ls = 0.f;
#pragma unroll
        for (int t = 0; t < 4; ++t) { cs[t] = __builtin_amdgcn_exp2f(ml[t].x - M); Ls = fmaf(cs[t], ml[t].y, Ls); }
        stat = __float_as_uint((lane & 2) ? Ls : M);
      }
      const unsigned vq = n16 >> 2, vp = n16 & 3, vf = (vq | (g4 & 1) << 2) << 1;
      const LAS unsigned char* vb = LB + LB_V + (8 * g4 + vq) * 512 + (vp & 1) * 8;
      const unsigned lf = ((vq >> 1) | (g4 & 1) << 1) << 1;
      const LAS unsigned char* lb = LB + LB_V + 49152 + w * 4096 + (8 * g4 + vq) * 128 + (vp & 1) * 8;
#define VTR(p_) __builtin_bit_cast(v2u, __builtin_amdgcn_ds_read_tr16_b64_v4i16((LAS v4s*)(p_)))
#define V4(x_, y_) v4u{(x_).x, (x_).y, (y_).x, (y_).y}
      const v4f zero4 = {0.f, 0.f, 0.f, 0.f};
      float o01 = 0.f, o23 = 0.f;
      lds_wait_ge<false>(FL + F_LANDED, 112u);
#pragma unroll
      for (int t = 0; t < 3; ++t) {
        v2u x[4][2];
#pragma unroll
        for (int u = 0; u < 4; ++u) { const LAS unsigned char* p_ = vb + (((8 * w + 2 * u + (vp >> 1)) ^ vf) << 4) + t * 16384; x[u][0] = VTR(p_); x[u][1] = VTR(p_ + 2048); }
        const v4f da = mfma(pb[t], V4(x[1][0], x[1][1]), mfma(pa[t], V4(x[0][0], x[0][1]), zero4));
        const v4f db = mfma(pb[t], V4(x[3][0], x[3][1]), mfma(pa[t], V4(x[2][0], x[2][1]), zero4));
        o01 = fmaf(cs[t], da.x, o01); o23 = fmaf(cs[t], db.x, o23);
      }
      const unsigned oaddr = WS_O + ((h * 32 + hd * 16 + 4 * w + (g4 >> 1)) * 32 + s) * 128 + n16 * 8;
      asm volatile("" : "+v"(o01), "+v"(o23));
      VMWAIT(0);
      {
        v2u x[4][2];
#pragma unroll
        for (int u = 0; u < 4; ++u) { const LAS unsigned char* p_ = lb + (((2 * u + (vp >> 1)) ^ lf) << 4); x[u][0] = VTR(p_); x[u][1] = VTR(p_ + 512); }
        const v4f da = mfma(pb[3], V4(x[1][0], x[1][1]), mfma(pa[3], V4(x[0][0], x[0][1]), zero4));
        const v4f db = mfma(pb[3], V4(x[3][0], x[3][1]), mfma(pa[3], V4(x[2][0], x[2][1]), zero4));
        o01 = fmaf(cs[3], da.x, o01); o23 = fmaf(cs[3], db.x, o23);
        const unsigned ga = cvtpk(o01, dpp<XOR1>(o01)), gb = cvtpk(o23, dpp<XOR1>(o23));
        __builtin_amdgcn_raw_buffer_store_b64(v2u{(lane & 1) ? stat : ga, tag}, rw, oaddr, 0, AUX_SC1);
        __builtin_amdgcn_raw_buffer_store_b64(v2u{(lane & 1) ? stat : gb, tag}, rw, oaddr, 2 * 4096, AUX_SC1);
      }
    } else if (w == 4) {
      const bool mlp = b < 224;
      __builtin_amdgcn_s_setprio(3);
      const unsigned off16 = lane < 56 ? lane * 16 : OOB;
      const v4u xa = __builtin_amdgcn_raw_buffer_load_b128(mkrs(a.x, D * 2), off16, 0, 0);
      const v4u gv = __builtin_amdgcn_raw_buffer_load_b128(mkrs(a.gw, D * 2), off16, 0, 0);
      const unsigned b1v = __builtin_amdgcn_raw_buffer_load_b16(mkrs(a.b1 + (mlp ? 8 * b : 0), 16), c8 * 2, 0, 0);
      const unsigned b2v = __builtin_amdgcn_raw_buffer_load_b16(mkrs(a.b2, D * 2), b < 14 ? ((b % 7) * 64 + ((lane & 31) >> 2) * 8 + (b / 7) * 4 + (lane & 3)) * 2 : OOB, 0, 0);
      float gs = 0.f;
#pragma unroll
      for (int e = 0; e < 4; ++e) gs += rbf(bflo(xa[e]) * bflo(gv[e])) + rbf(bfhi(xa[e]) * bfhi(gv[e]));
      gs = tr32(gs, gs); gs = tr16(gs, gs); gs += dpp<ROR8>(gs); gs += dpp<XOR7>(gs); gs += dpp<XOR2>(gs); gs += dpp<XOR1>(gs);
      const float gate = rbf(1.f / (1.f + __expf(-rbf(gs))));
      if (lane < 32) ((LAS float*)(LB + LB_FIN))[256 + lane] = (1.f - gate) * bflo(b2v);
      if (lane == 0) *(LAS float*)(LB + LB_MO + 1792) = gate;
      lds_wait_ge<true>(FL + F_Q, 1u);
      const unsigned ho = mlp ? WS_H + b * 512 + lane * 8 : OOB;
      v2u hg = __builtin_amdgcn_raw_buffer_load_b64(rw, ho, 0, AUX_SC1);
      lds_wait_or_time(FL + F_LANDED, 112u, tstart, WTIME);
      __builtin_amdgcn_s_setprio(1);
      __builtin_amdgcn_s_sleep(WSLEEP);
      const __amdgpu_buffer_rsrc_t r2 = mkrs(a.w2 + (size_t)(mlp ? 8 * b : 0) * D, mlp ? 8 * D * 2 : 0u);
      v4u w2v[8];
#pragma unroll
      for (int r = 0; r < 8; ++r) w2v[r] = LDNT(r2, off16, r * (D * 2));
      __builtin_amdgcn_sched_barrier(0);
      float hv = 0.f;
      if (mlp) {
        if (!__all(hg.y == tag)) {
          unsigned spins = 0;
#pragma nounroll
          do { CBAR(); hg = __builtin_amdgcn_raw_buffer_load_b64(rw, ho, 0, AUX_SC1); } while (!__all(hg.y == tag) && ++spins < SPIN_CAP);
        }
        hv = __uint_as_float(hg.x);
        hv += dpp<ROR8>(hv); hv = tr16(hv, hv); hv = tr32(hv, hv);
        hv = rbf(rbf(hv) + bflo(b1v));
        hv = hv > 0.f ? hv : 0.f;
        hv = rbf(hv * hv) * (1.f - gate);
      }
      float mo[8];
#pragma unroll
      for (int e = 0; e < 8; ++e) mo[e] = 0.f;
#pragma unroll
      for (int u = 0; u < 8; ++u) {
        const float hu = __uint_as_float(__builtin_amdgcn_readlane(__float_as_uint(hv), u));
#pragma unroll
        for (int e = 0; e < 4; ++e) { mo[2 * e] = fmaf(hu, bflo(w2v[u][e]), mo[2 * e]); mo[2 * e + 1] = fmaf(hu, bfhi(w2v[u][e]), mo[2 * e + 1]); }
      }
      if (lane < 56) { *(LAS v4f*)(LB + LB_MO + lane * 32) = v4f{mo[0], mo[1], mo[2], mo[3]}; *(LAS v4f*)(LB + LB_MO + lane * 32 + 16) = v4f{mo[4], mo[5], mo[6], mo[7]}; }
      lds_set(FL + F_G, 1u, lane);
    }
    if (b < 14) {
      if (w == 7) {
        lds_wait_ge<true>(FL + F_LANDED, 112u);
        u64 pc1; unsigned keep1;
        asm volatile("s_getpc_b64 %0" : "=s"(pc1));
        asm volatile("s_mov_b32 %0, m0\n\ts_mov_b32 m0, %3\n\ts_nop 0\n\tglobal_load_lds_dword %1, %2\n\ts_mov_b32 m0, %0"
                     : "=&s"(keep1) : "v"((lane < 12 ? lane : 12) * 128), "s"(pc1 & ~127ull), "s"(ldsbase + LB_SCRAP) : "memory");
      }
      lds_wait_ge<true>(FL + F_ODONE, 1u);
      const unsigned fo = WS_O + (w * 32 + rg) * 4096 + b * 128 + c8 * 16;
      const int cl = lane & 31;
      LAS float* fin = (LAS float*)(LB + LB_FIN);
      GAS u16* op = (GAS u16*)(a.out + (b % 7) * 64 + (cl >> 2) * 8 + (b / 7) * 4 + (cl & 3));
      LAS float* fw = (lane & 8) ? (LAS float*)(LB + LB_SCRAP) + lane : fin + w * 32 + c8 * 4 + (lane >> 5) * 2 + ((lane >> 4) & 1);
      LAS unsigned* tk = lane == 0 ? FL + F_FIN : (LAS unsigned*)(LB + LB_SCRAP) + lane;
      unsigned LO = 0x00003f80u, HI = 0x3f800000u;
      float bias = fin[256 + cl];
      const LAS float* fr = fin + (lane >> 5) * 128 + cl;
      asm volatile("" : "+v"(fw), "+v"(tk), "+v"(op), "+s"(LO), "+s"(HI), "+v"(bias), "+v"(fr));
      v4u g[4];
      gather4<32768>(rw, fo, ~tag, g);
      float s0 = 0.f, s1 = 0.f, s2 = 0.f, s3 = 0.f;
#pragma unroll
      for (int i = 0; i < 4; ++i) { s0 = dot2(g[i].x, LO, s0); s1 = dot2(g[i].x, HI, s1); s2 = dot2(g[i].z, LO, s2); s3 = dot2(g[i].z, HI, s3); }
      float t = tr16(tr32(s0, s2), tr32(s1, s3));
      t += dpp<ROR8>(t);
      *fw = t;
      CBAR();
      unsigned tkv;
      asm volatile("ds_add_rtn_u32 %0, %1, %2\n\ts_waitcnt lgkmcnt(0)" : "=&v"(tkv) : "v"(tk), "v"(1u) : "memory");
      if (__builtin_amdgcn_readfirstlane(tkv) == 7u) {
        float r = (fr[0] + fr[32]) + (fr[64] + fr[96]);
        r = tr32(r, r) + bias;
        if (lane < 32) *op = (u16)cvtpk(r, 0.f);
        if (b == 0 && lane == 0) __hip_atomic_store((GAS unsigned*)(a.ws + WS_EPOCH), epoch + 1u, RLX_AGENT);
      }
    }
  } else {
    __builtin_amdgcn_s_setprio(3);
    const unsigned off16 = lane < 56 ? lane * 16 : OOB;
    const __amdgpu_buffer_rsrc_t rw = mkrs(a.ws, WS_END);
    lds_wait_ge<true>(FL + F_QP, 9u);
    __builtin_amdgcn_s_sleep(QDELAY);
    {
      unsigned t_, n_;
      asm volatile(
          "buffer_load_dwordx4 v[64:67], %[qo], %[srd], 0 offen sc1\n\t"
          "buffer_load_dwordx4 v[68:71], %[qo], %[srd], 0 offen offset:1024 sc1\n\t"
          "buffer_load_dwordx4 v[72:75], %[qo], %[srd], 0 offen offset:2048 sc1\n\t"
          "buffer_load_dwordx4 v[76:79], %[qo], %[srd], 0 offen offset:3072 sc1\n\t"
          "buffer_load_dwordx4 v[80:83], %[qo], %[srd], %[k4] offen sc1\n\t"
          "buffer_load_dwordx4 v[84:87], %[qo], %[srd], %[k4] offen offset:1024 sc1\n\t"
          "buffer_load_dwordx4 v[88:91], %[qo], %[srd], %[k4] offen offset:2048 sc1\n\t"
          "buffer_load_dwordx4 v[92:95], %[qo], %[srd], %[k4] offen offset:3072 sc1\n\t"
          "s_sleep " STR(QSLEEP) "\n\t"
          "buffer_load_dwordx4 v[96:99], %[qo], %[srd], 0 offen sc1\n\t"
          "buffer_load_dwordx4 v[100:103], %[qo], %[srd], 0 offen offset:1024 sc1\n\t"
          "buffer_load_dwordx4 v[104:107], %[qo], %[srd], 0 offen offset:2048 sc1\n\t"
          "buffer_load_dwordx4 v[108:111], %[qo], %[srd], 0 offen offset:3072 sc1\n\t"
          "buffer_load_dwordx4 v[112:115], %[qo], %[srd], %[k4] offen sc1\n\t"
          "buffer_load_dwordx4 v[116:119], %[qo], %[srd], %[k4] offen offset:1024 sc1\n\t"
          "buffer_load_dwordx4 v[120:123], %[qo], %[srd], %[k4] offen offset:2048 sc1\n\t"
          "buffer_load_dwordx4 v[124:127], %[qo], %[srd], %[k4] offen offset:3072 sc1\n\t"
          "s_mov_b32 %[n], 0x10000\n\t"
          ".Lq%=:\n"
          "s_waitcnt vmcnt(8)\n\t"
          "v_xor_b32 %[t], %[tag], v65\n\t"
          "v_bitop3_b32 %[t], v67, %[tag], %[t] bitop3:0xbe\n\t"
          "v_bitop3_b32 %[t], v69, %[tag], %[t] bitop3:0xbe\n\t"
          "v_bitop3_b32 %[t], v71, %[tag], %[t] bitop3:0xbe\n\t"
          "v_bitop3_b32 %[t], v73, %[tag], %[t] bitop3:0xbe\n\t"
          "v_bitop3_b32 %[t], v75, %[tag], %[t] bitop3:0xbe\n\t"
          "v_bitop3_b32 %[t], v77, %[tag], %[t] bitop3:0xbe\n\t"
          "v_bitop3_b32 %[t], v79, %[tag], %[t] bitop3:0xbe\n\t"
          "v_bitop3_b32 %[t], v81, %[tag], %[t] bitop3:0xbe\n\t"
          "v_bitop3_b32 %[t], v83, %[tag], %[t] bitop3:0xbe\n\t"
          "v_bitop3_b32 %[t], v85, %[tag], %[t] bitop3:0xbe\n\t"
          "v_bitop3_b32 %[t], v87, %[tag], %[t] bitop3:0xbe\n\t"
          "v_bitop3_b32 %[t], v89, %[tag], %[t] bitop3:0xbe\n\t"
          "v_bitop3_b32 %[t], v91, %[tag], %[t] bitop3:0xbe\n\t"
          "v_bitop3_b32 %[t], v93, %[tag], %[t] bitop3:0xbe\n\t"
          "v_bitop3_b32 %[t], v95, %[tag], %[t] bitop3:0xbe\n\t"
          "v_cmp_ne_u32 vcc, 0, %[t]\n\t"
          "s_cbranch_vccz .Lqa%=\n\t"
          "buffer_load_dwordx4 v[64:67], %[qo], %[srd], 0 offen sc1\n\t"
          "buffer_load_dwordx4 v[68:71], %[qo], %[srd], 0 offen offset:1024 sc1\n\t"
          "buffer_load_dwordx4 v[72:75], %[qo], %[srd], 0 offen offset:2048 sc1\n\t"
          "buffer_load_dwordx4 v[76:79], %[qo], %[srd], 0 offen offset:3072 sc1\n\t"
          "buffer_load_dwordx4 v[80:83], %[qo], %[srd], %[k4] offen sc1\n\t"
          "buffer_load_dwordx4 v[84:87], %[qo], %[srd], %[k4] offen offset:1024 sc1\n\t"
          "buffer_load_dwordx4 v[88:91], %[qo], %[srd], %[k4] offen offset:2048 sc1\n\t"
          "buffer_load_dwordx4 v[92:95], %[qo], %[srd], %[k4] offen offset:3072 sc1\n\t"
          "s_waitcnt vmcnt(8)\n\t"
          "v_xor_b32 %[t], %[tag], v97\n\t"
          "v_bitop3_b32 %[t], v99, %[tag], %[t] bitop3:0xbe\n\t"
          "v_bitop3_b32 %[t], v101, %[tag], %[t] bitop3:0xbe\n\t"
          "v_bitop3_b32 %[t], v103, %[tag], %[t] bitop3:0xbe\n\t"
          "v_bitop3_b32 %[t], v105, %[tag], %[t] bitop3:0xbe\n\t"
          "v_bitop3_b32 %[t], v107, %[tag], %[t] bitop3:0xbe\n\t"
          "v_bitop3_b32 %[t], v109, %[tag], %[t] bitop3:0xbe\n\t"
          "v_bitop3_b32 %[t], v111, %[tag], %[t] bitop3:0xbe\n\t"
          "v_bitop3_b32 %[t], v113, %[tag], %[t] bitop3:0xbe\n\t"
          "v_bitop3_b32 %[t], v115, %[tag], %[t] bitop3:0xbe\n\t"
          "v_bitop3_b32 %[t], v117, %[tag], %[t] bitop3:0xbe\n\t"
          "v_bitop3_b32 %[t], v119, %[tag], %[t] bitop3:0xbe\n\t"
          "v_bitop3_b32 %[t], v121, %[tag], %[t] bitop3:0xbe\n\t"
          "v_bitop3_b32 %[t], v123, %[tag], %[t] bitop3:0xbe\n\t"
          "v_bitop3_b32 %[t], v125, %[tag], %[t] bitop3:0xbe\n\t"
          "v_bitop3_b32 %[t], v127, %[tag], %[t] bitop3:0xbe\n\t"
          "v_cmp_ne_u32 vcc, 0, %[t]\n\t"
          "s_cbranch_vccz .Lqb%=\n\t"
          "buffer_load_dwordx4 v[96:99], %[qo], %[srd], 0 offen sc1\n\t"
          "buffer_load_dwordx4 v[100:103], %[qo], %[srd], 0 offen offset:1024 sc1\n\t"
          "buffer_load_dwordx4 v[104:107], %[qo], %[srd], 0 offen offset:2048 sc1\n\t"
          "buffer_load_dwordx4 v[108:111], %[qo], %[srd], 0 offen offset:3072 sc1\n\t"
          "buffer_load_dwordx4 v[112:115], %[qo], %[srd], %[k4] offen sc1\n\t"
          "buffer_load_dwordx4 v[116:119], %[qo], %[srd], %[k4] offen offset:1024 sc1\n\t"
          "buffer_load_dwordx4 v[120:123], %[qo], %[srd], %[k4] offen offset:2048 sc1\n\t"
          "buffer_load_dwordx4 v[124:127], %[qo], %[srd], %[k4] offen offset:3072 sc1\n\t"
          "s_sub_u32 %[n], %[n], 1\n\t"
          "s_cbranch_scc0 .Lq%=\n\t"
          "s_waitcnt vmcnt(0)\n\t"
          ".Lqa%=:\n"
          "ds_write2_b32 %[la], v64, v66 offset0:0 offset1:1\n\t"
          "ds_write2_b32 %[la], v68, v70 offset0:32 offset1:33\n\t"
          "ds_write2_b32 %[la], v72, v74 offset0:64 offset1:65\n\t"
          "ds_write2_b32 %[la], v76, v78 offset0:96 offset1:97\n\t"
          "ds_write2_b32 %[la], v80, v82 offset0:132 offset1:133\n\t"
          "ds_write2_b32 %[la], v84, v86 offset0:164 offset1:165\n\t"
          "ds_write2_b32 %[la], v88, v90 offset0:196 offset1:197\n\t"
          "ds_write2_b32 %[la], v92, v94 offset0:228 offset1:229\n\t"
          "s_branch .Lqd%=\n\t"
          ".Lqb%=:\n"
          "ds_write2_b32 %[la], v96, v98 offset0:0 offset1:1\n\t"
          "ds_write2_b32 %[la], v100, v102 offset0:32 offset1:33\n\t"
          "ds_write2_b32 %[la], v104, v106 offset0:64 offset1:65\n\t"
          "ds_write2_b32 %[la], v108, v110 offset0:96 offset1:97\n\t"
          "ds_write2_b32 %[la], v112, v114 offset0:132 offset1:133\n\t"
          "ds_write2_b32 %[la], v116, v118 offset0:164 offset1:165\n\t"
          "ds_write2_b32 %[la], v120, v122 offset0:196 offset1:197\n\t"
          "ds_write2_b32 %[la], v124, v126 offset0:228 offset1:229\n\t"
          ".Lqd%=:\n"
          "v_mov_b32 %[t], 1\n\t"
          "ds_write_b32 %[fa], %[t]\n\t"
          "s_waitcnt vmcnt(0)\n\t"
          : [t] "=&v"(t_), [n] "=&s"(n_)
          : [qo] "v"(WS_Q + h * 8192 + lane * 16), [srd] "s"(mksrd(a.ws, WS_END)), [k4] "s"(4096), [tag] "s"(tag),
            [la] "v"(ldsbase + LB_Q + (lane >> 4) * 1056 + (lane & 15) * 8), [fa] "v"(lane == 0 ? ldsbase + LB_FLG + F_Q * 4 : ldsbase + LB_SCRAP + lane * 4)
          : "memory", "scc", "vcc", "v64", "v65", "v66", "v67", "v68", "v69", "v70", "v71", "v72", "v73", "v74", "v75", "v76", "v77", "v78", "v79", "v80", "v81", "v82", "v83", "v84", "v85", "v86", "v87", "v88", "v89", "v90", "v91", "v92", "v93", "v94", "v95", "v96", "v97", "v98", "v99", "v100", "v101", "v102", "v103", "v104", "v105", "v106", "v107", "v108", "v109", "v110", "v111", "v112", "v113", "v114", "v115", "v116", "v117", "v118", "v119", "v120", "v121", "v122", "v123", "v124", "v125", "v126", "v127");
    }
    lds_wait_or_time(FL + F_LANDED, 112u, tstart, WTIME);
    __builtin_amdgcn_s_setprio(1);
    __builtin_amdgcn_s_sleep(WSLEEP);
    const __amdgpu_buffer_rsrc_t ro = mkrs(a.wo + (size_t)(h * 512 + s * 16) * D, 16 * D * 2);
    v4u wv[16];
#pragma unroll
    for (int r = 0; r < 16; ++r) wv[r] = LDNT(ro, off16, r * (D * 2));
    __builtin_amdgcn_sched_barrier(0);
    unsigned wp[8][8];
#pragma unroll
    for (int pr = 0; pr < 8; ++pr)
#pragma unroll
      for (int e = 0; e < 4; ++e) {
        wp[pr][2 * e] = __builtin_amdgcn_perm(wv[2 * pr + 1][e], wv[2 * pr][e], 0x05040100u);
        wp[pr][2 * e + 1] = __builtin_amdgcn_perm(wv[2 * pr + 1][e], wv[2 * pr][e], 0x07060302u);
      }
#pragma unroll
    for (int pr = 0; pr < 8; ++pr) asm volatile("" : "+v"(wp[pr][0]), "+v"(wp[pr][1]), "+v"(wp[pr][2]), "+v"(wp[pr][3]), "+v"(wp[pr][4]), "+v"(wp[pr][5]), "+v"(wp[pr][6]), "+v"(wp[pr][7]));
    __builtin_amdgcn_sched_barrier(0);
    lds_wait_ge<false>(FL + F_G, 1u);
    const v4f m0 = *(const LAS v4f*)(LB + LB_MO + (lane < 56 ? lane : 0) * 32), m1 = *(const LAS v4f*)(LB + LB_MO + (lane < 56 ? lane : 0) * 32 + 16);
    const float gate = *(const LAS float*)(LB + LB_MO + 1792);
    float acc[8] = {m0.x, m0.y, m0.z, m0.w, m1.x, m1.y, m1.z, m1.w};
    unsigned fo = lane < 56 ? WS_O + (h * 32 + s) * 4096 + lane * 16 : OOB;
    float gate_ = gate;
    asm volatile("" : "+v"(acc[0]), "+v"(acc[1]), "+v"(acc[2]), "+v"(acc[3]), "+v"(acc[4]), "+v"(acc[5]), "+v"(acc[6]), "+v"(acc[7]), "+v"(fo), "+v"(gate_));
    unsigned opk;
    {
      v4u g[4];
      gather4<1024>(rw, WS_O + (h * 32 + s) * 4096 + lane * 16, tag, g);
      float z[4];
#pragma unroll
      for (int i = 0; i < 4; ++i) z[i] = __uint_as_float(g[i].z);
      float M = vmax(vmax3(z[0], z[1], z[2]), z[3]);
      M = vmax(M, dpp<ROR8>(M)); M = swapmax32(swapmax16(M));
      float Lt = 0.f, o0 = 0.f, o1 = 0.f;
#pragma unroll
      for (int i = 0; i < 4; ++i) { const float wg = dpp<0xA0>(__builtin_amdgcn_exp2f(z[i] - M));
        Lt = fmaf(wg, z[i], Lt); o0 = fmaf(wg, bflo(g[i].x), o0); o1 = fmaf(wg, bfhi(g[i].x), o1); }
      float t = tr32(o0, o1);
      Lt = tr32(Lt, Lt);
      t = tr16(t, t); Lt = tr16(Lt, Lt);
      t += dpp<ROR8>(t); Lt += dpp<ROR8>(Lt);
      t *= gate_ * __builtin_amdgcn_rcpf(dpp<0xF5>(Lt));
      auto r = __builtin_amdgcn_permlane32_swap(__float_as_uint(t), __float_as_uint(t), false, false);
      opk = cvtpk(__uint_as_float(r[0]), __uint_as_float(r[1]));
    }
    unsigned ops[8];
#pragma unroll
    for (int pr = 0; pr < 8; ++pr) ops[pr] = __builtin_amdgcn_readlane(opk, pr);
    __builtin_amdgcn_sched_barrier(0);
#pragma unroll
    for (int pr = 0; pr < 8; ++pr)
#pragma unroll
      for (int e = 0; e < 8; ++e) acc[e] = dot2(wp[pr][e], ops[pr], acc[e]);
    {
      const v4u p0 = {cvtpk(acc[0], acc[1]), ~tag, cvtpk(acc[2], acc[3]), ~tag}, p1 = {cvtpk(acc[4], acc[5]), ~tag, cvtpk(acc[6], acc[7]), ~tag};
      STSC(p0, rw, fo, 0);
      STSC(p1, rw, fo, 7 * 128);
      lds_set(FL + F_ODONE, 1u, lane);
    }
  }
}

extern "C" void kernel_launch(void* const* d_in, const int* in_sizes, int n_in, void* d_out, int out_size, void* d_ws, size_t ws_size, hipStream_t stream) {
  Args a;
  a.x = (const u16*)d_in[0]; a.kc = (const u16*)d_in[1]; a.vc = (const u16*)d_in[2]; a.wq = (const u16*)d_in[3]; a.wo = (const u16*)d_in[4];
  a.w1 = (const u16*)d_in[6]; a.b1 = (const u16*)d_in[7]; a.w2 = (const u16*)d_in[8]; a.b2 = (const u16*)d_in[9]; a.gw = (const u16*)d_in[10];
  a.out = (u16*)d_out; a.ws = (unsigned char*)d_ws;
  hipLaunchKernelGGL(fused_decode, dim3(NWG), dim3(NTHR), 0, stream, a);
}
```
